```python
import torch

def _softmax(x, axis=-1):
    return torch.softmax(x, dim=axis)
def _gelu(x):
    return torch.nn.functional.gelu(x, approximate="tanh")
def _silu(x):
    return torch.nn.functional.silu(x)
def _sigmoid(x):
    return torch.sigmoid(x)
def _layernorm(x, axis=-1, eps=1e-5):
    m = x.mean(dim=axis, keepdim=True)
    v = x.var(dim=axis, keepdim=True, unbiased=False)
    return (x - m) / torch.sqrt(v + eps)
def _scatter_add(tgt, idx, src, axis=0):
    return tgt.clone().index_add_(axis, idx, src)
def _segment_sum(src, seg, n):
    out = torch.zeros((n,) + src.shape[1:], dtype=src.dtype)
    return out.index_add_(0, seg, src)
def _pad(x, pw):
    flat = tuple(v for pair in reversed(pw) for v in pair)
    return torch.nn.functional.pad(x, flat)

kernel_name = 'concat_heads__gqa_block_cached__mha_block_cached__near_ridge'

def setup_inputs():

    _w = (lambda a: (lambda t: t.to(torch.bfloat16) if t.is_floating_point() else t)(torch.as_tensor(a)))
    def _A_setup():
        B, Sq, D = 16, 32, 1024
        Hq, Hkv, Sk, Dh = 32, 4, 512, 128
        assert Hq % Hkv == 0, (Hq, Hkv)
        return {
            "x": _w((lambda *s: torch.randn(*s, dtype=torch.bfloat16) * 0.8)(B, Sq, D)),
            "k_cache": _w((lambda *s: torch.randn(*s, dtype=torch.bfloat16) * 0.8)(B, Hkv, Sk, Dh)),
            "v_cache": _w((lambda *s: torch.randn(*s, dtype=torch.bfloat16) * 0.05)(B, Hkv, Sk, Dh)),
            "wq": _w((lambda *s: torch.randn(*s, dtype=torch.bfloat16) * 0.8)(D, Hq * Dh) * (D ** -0.5)),
            "wo": _w((lambda *s: torch.randn(*s, dtype=torch.bfloat16) * 0.05)(Hq * Dh, D) * ((Hq * Dh) ** -0.5)),
        }
    _A_in = _A_setup()

    def _B_setup():
        B, Sq, D = 16, 32, 1024
        H, Sk, Dh = 32, 512, 128
        return {
            "x": _w((lambda *s: torch.randn(*s, dtype=torch.bfloat16) * 0.8)(B, Sq, D)),
            "k_cache": _w((lambda *s: torch.randn(*s, dtype=torch.bfloat16) * 0.8)(B, H, Sk, Dh)),
            "v_cache": _w((lambda *s: torch.randn(*s, dtype=torch.bfloat16) * 0.05)(B, H, Sk, Dh)),
            "wq": _w((lambda *s: torch.randn(*s, dtype=torch.bfloat16) * 0.8)(D, H * Dh) * (D ** -0.5)),
            "wo": _w((lambda *s: torch.randn(*s, dtype=torch.bfloat16) * 0.05)(H * Dh, D) * ((H * Dh) ** -0.5)),
        }
    _B_in = _B_setup()

    return {
        "A_x": _A_in["x"],
        "A_k_cache": _A_in["k_cache"],
        "A_v_cache": _A_in["v_cache"],
        "A_wq": _A_in["wq"],
        "A_wo": _A_in["wo"],
        "B_x": _B_in["x"],
        "B_k_cache": _B_in["k_cache"],
        "B_v_cache": _B_in["v_cache"],
        "B_wq": _B_in["wq"],
        "B_wo": _B_in["wo"],
    }

def reference(A_x, A_k_cache, A_v_cache, A_wq, A_wo, B_x, B_k_cache, B_v_cache, B_wq, B_wo):
    def _A_ref(x, k_cache, v_cache, wq, wo):
        B, Sq, D = x.shape
        Hq, Dh = 32, 128
        Hkv = k_cache.shape[1]
        rep = Hq // Hkv
        q = (lambda x, *d: x.permute(*d))((x @ wq).reshape(B, Sq, Hq, Dh), 0, 2, 1, 3)
        k = (lambda x, n, axis: torch.repeat_interleave(x, n, dim=axis))(k_cache, rep, axis=1)
        v = (lambda x, n, axis: torch.repeat_interleave(x, n, dim=axis))(v_cache, rep, axis=1)
        scores = torch.einsum("bhqd,bhkd->bhqk", q, k) * (Dh ** -0.5)
        p = _softmax(scores, axis=-1)
        o = torch.einsum("bhqk,bhkd->bhqd", p, v)
        return (lambda x, *d: x.permute(*d))(o, 0, 2, 1, 3).reshape(B, Sq, Hq * Dh) @ wo

    def _B_ref(x, k_cache, v_cache, wq, wo):
        B, Sq, D = x.shape
        H, Dh = 32, 128
        q = (lambda x, *d: x.permute(*d))((x @ wq).reshape(B, Sq, H, Dh), 0, 2, 1, 3)
        scores = torch.einsum("bhqd,bhkd->bhqk", q, k_cache) * (Dh ** -0.5)
        p = _softmax(scores, axis=-1)
        o = torch.einsum("bhqk,bhkd->bhqd", p, v_cache)
        return (lambda x, *d: x.permute(*d))(o, 0, 2, 1, 3).reshape(B, Sq, H * Dh) @ wo

    a = _A_ref(A_x, A_k_cache, A_v_cache, A_wq, A_wo)
    b = _B_ref(B_x, B_k_cache, B_v_cache, B_wq, B_wo)
    return torch.concatenate([a, b], axis=-1)
```

```cpp
#include <hip/hip_runtime.h>
#include <hip/hip_bf16.h>
#include <stdint.h>

typedef unsigned short u16;
__device__ __forceinline__ float bf2f(u16 v) { return __uint_as_float(((unsigned)v) << 16); }
__device__ __forceinline__ u16 f2bf(float f) { __hip_bfloat16 b = __float2bfloat16(f); return *reinterpret_cast<u16*>(&b); }

__global__ void gemm_naive(const u16* __restrict__ A, const u16* __restrict__ Bm, u16* __restrict__ C, int M, int N, int K, int ldc) {
    int n = blockIdx.x * 256 + threadIdx.x, m = blockIdx.y;
    float acc = 0.f;
    for (int k = 0; k < K; ++k) acc += bf2f(A[(size_t)m * K + k]) * bf2f(Bm[(size_t)k * N + n]);
    C[(size_t)m * ldc + n] = f2bf(acc);
}

__global__ void attn_naive(const u16* __restrict__ q, const u16* __restrict__ kc, const u16* __restrict__ vc, u16* __restrict__ o, int Hkv) {
    __shared__ float sc[512];
    __shared__ float qs[128];
    __shared__ float red[256];
    int s = blockIdx.x, h = blockIdx.y, b = blockIdx.z, t = threadIdx.x;
    int hk = h / (32 / Hkv);
    const u16* K = kc + ((size_t)b * Hkv + hk) * 512 * 128;
    const u16* V = vc + ((size_t)b * Hkv + hk) * 512 * 128;
    if (t < 128) qs[t] = bf2f(q[((size_t)b * 32 + s) * 4096 + h * 128 + t]);
    __syncthreads();
    float mx = -1e30f;
    for (int j = t; j < 512; j += 256) {
        float a = 0.f;
        for (int d = 0; d < 128; ++d) a += qs[d] * bf2f(K[(size_t)j * 128 + d]);
        a *= 0.08838834764831845f;
        sc[j] = a; mx = fmaxf(mx, a);
    }
    red[t] = mx; __syncthreads();
    for (int st = 128; st > 0; st >>= 1) { if (t < st) red[t] = fmaxf(red[t], red[t + st]); __syncthreads(); }
    mx = red[0]; __syncthreads();
    float sum = 0.f;
    for (int j = t; j < 512; j += 256) { float e = __expf(sc[j] - mx); sc[j] = e; sum += e; }
    red[t] = sum; __syncthreads();
    for (int st = 128; st > 0; st >>= 1) { if (t < st) red[t] += red[t + st]; __syncthreads(); }
    sum = red[0];
    if (t < 128) {
        float a = 0.f;
        for (int j = 0; j < 512; ++j) a += sc[j] * bf2f(V[(size_t)j * 128 + t]);
        o[((size_t)b * 32 + s) * 4096 + h * 128 + t] = f2bf(a / sum);
    }
}

extern "C" void kernel_launch(void* const* d_in, const int* in_sizes, int n_in,
                              void* d_out, int out_size, void* d_ws, size_t ws_size,
                              hipStream_t stream) {
    u16* ws = (u16*)d_ws;
    u16* out = (u16*)d_out;
    for (int br = 0; br < 2; ++br) {
        const u16* x = (const u16*)d_in[br * 5 + 0];
        const u16* kc = (const u16*)d_in[br * 5 + 1];
        const u16* vc = (const u16*)d_in[br * 5 + 2];
        const u16* wq = (const u16*)d_in[br * 5 + 3];
        const u16* wo = (const u16*)d_in[br * 5 + 4];
        u16* q = ws + (size_t)br * 2 * 512 * 4096;
        u16* o = q + (size_t)512 * 4096;
        gemm_naive<<<dim3(16, 512), 256, 0, stream>>>(x, wq, q, 512, 4096, 1024, 4096);
        attn_naive<<<dim3(32, 32, 16), 256, 0, stream>>>(q, kc, vc, o, br == 0 ? 4 : 32);
        gemm_naive<<<dim3(4, 512), 256, 0, stream>>>(o, wo, out + br * 1024, 512, 1024, 4096, 2048);
    }
}
```

```cpp
#include <hip/hip_runtime.h>
#include <hip/hip_bf16.h>
#include <stdint.h>

typedef unsigned short u16;
__device__ __forceinline__ float bf2f(u16 v) { return __uint_as_float(((unsigned)v) << 16); }
__device__ __forceinline__ u16 f2bf(float f) { __hip_bfloat16 b = __float2bfloat16(f); return *reinterpret_cast<u16*>(&b); }

__global__ void gemm_naive(const u16* __restrict__ A, const u16* __restrict__ Bm, u16* __restrict__ C, int M, int N, int K, int ldc) {
    int n = blockIdx.x * 256 + threadIdx.x, m = blockIdx.y;
    float acc = 0.f;
    for (int k = 0; k < K; ++k) acc += bf2f(A[(size_t)m * K + k]) * bf2f(Bm[(size_t)k * N + n]);
    C[(size_t)m * ldc + n] = f2bf(acc);
}

__global__ void attn_naive(const u16* __restrict__ q, const u16* __restrict__ kc, const u16* __restrict__ vc, u16* __restrict__ o, int Hkv) {
    __shared__ float sc[512];
    __shared__ float qs[128];
    __shared__ float red[256];
    int s = blockIdx.x, h = blockIdx.y, b = blockIdx.z, t = threadIdx.x;
    int hk = h / (32 / Hkv);
    const u16* K = kc + ((size_t)b * Hkv + hk) * 512 * 128;
    const u16* V = vc + ((size_t)b * Hkv + hk) * 512 * 128;
    if (t < 128) qs[t] = bf2f(q[((size_t)b * 32 + s) * 4096 + h * 128 + t]);
    __syncthreads();
    float mx = -1e30f;
    for (int j = t; j < 512; j += 256) {
        float a = 0.f;
        for (int d = 0; d < 128; ++d) a += qs[d] * bf2f(K[(size_t)j * 128 + d]);
        a *= 0.08838834764831845f;
        sc[j] = a; mx = fmaxf(mx, a);
    }
    red[t] = mx; __syncthreads();
    for (int st = 128; st > 0; st >>= 1) { if (t < st) red[t] = fmaxf(red[t], red[t + st]); __syncthreads(); }
    mx = red[0]; __syncthreads();
    float sum = 0.f;
    for (int j = t; j < 512; j += 256) { float e = __expf(sc[j] - mx); sc[j] = e; sum += e; }
    red[t] = sum; __syncthreads();
    for (int st = 128; st > 0; st >>= 1) { if (t < st) red[t] += red[t + st]; __syncthreads(); }
    sum = red[0];
    if (t < 128) {
        float a = 0.f;
        for (int j = 0; j < 512; ++j) a += sc[j] * bf2f(V[(size_t)j * 128 + t]);
        o[((size_t)b * 32 + s) * 4096 + h * 128 + t] = f2bf(a / sum);
    }
}


typedef unsigned v4u __attribute__((ext_vector_type(4)));
typedef unsigned long long u64;
__device__ __forceinline__ __amdgpu_buffer_rsrc_t mkrs(const void* p) {
    unsigned long long pb = (unsigned long long)p;
    unsigned lo = __builtin_amdgcn_readfirstlane((unsigned)pb), hi = __builtin_amdgcn_readfirstlane((unsigned)(pb >> 32));
    void* ub = (void*)(((unsigned long long)hi << 32) | lo);
    return __builtin_amdgcn_make_buffer_rsrc(ub, 0, 0x7fffffff, 0x00020000);
}
template <int W, int AUX>
__device__ __forceinline__ v4u stream(const v4u* wbase, int lane, int nit) {
    __amdgpu_buffer_rsrc_t rs = mkrs(wbase);
    int vo = lane * 16;
    v4u a[W];
    v4u acc = {0, 0, 0, 0};
#pragma unroll
    for (int i = 0; i < W; ++i) a[i] = __builtin_amdgcn_raw_buffer_load_b128(rs, vo, i * 1024, AUX);
    for (int it = W; it < nit; it += W) {
#pragma unroll
        for (int i = 0; i < W; ++i) {
            acc ^= a[i]; asm volatile("" : "+v"(acc));
            a[i] = __builtin_amdgcn_raw_buffer_load_b128(rs, vo, (it + i) * 1024, AUX);
            __builtin_amdgcn_sched_barrier(0);
        }
    }
#pragma unroll
    for (int i = 0; i < W; ++i) acc ^= a[i];
    return acc;
}
struct Ptrs { const v4u* p[8]; int kb_per_wave[8]; };
template <int AUX>
__global__ __launch_bounds__(512, 2) void kstream(Ptrs P, int np, u64* stamps) {
    u64 t0 = __builtin_amdgcn_s_memrealtime();
    int lane = threadIdx.x & 63, w = __builtin_amdgcn_readfirstlane(threadIdx.x >> 6);
    v4u acc = {0,0,0,0};
#pragma unroll
    for (int i = 0; i < 8; ++i) if (i < np) acc ^= stream<4, AUX>(P.p[i] + ((size_t)blockIdx.x * 8 + w) * P.kb_per_wave[i] * 64, lane, P.kb_per_wave[i]);
    unsigned r = acc.x ^ acc.y ^ acc.z ^ acc.w;
    if (r == 0x12345678u) stamps[5000] = r;
    __syncthreads();
    u64 t1 = __builtin_amdgcn_s_memrealtime();
    if (threadIdx.x == 0) { stamps[blockIdx.x * 2] = t0; stamps[blockIdx.x * 2 + 1] = t1; }
}
__global__ void kdelay(const u64* stamps, int mult) {
    u64 mn = ~0ull, mx = 0;
    for (int i = 0; i < 256; ++i) { u64 a = stamps[2 * i], b = stamps[2 * i + 1]; mn = a < mn ? a : mn; mx = b > mx ? b : mx; }
    u64 d = (mx - mn) * mult; if (d > 2000000ull) d = 2000000ull;
    u64 t0 = __builtin_amdgcn_s_memrealtime();
    while (__builtin_amdgcn_s_memrealtime() - t0 < d) __builtin_amdgcn_s_sleep(10);
}

extern "C" void kernel_launch(void* const* d_in, const int* in_sizes, int n_in,
                              void* d_out, int out_size, void* d_ws, size_t ws_size,
                              hipStream_t stream) {
    Ptrs P; for (int i = 0; i < 8; ++i) { P.p[i] = (const v4u*)d_in[1]; P.kb_per_wave[i] = 4; }
    P.p[0] = (const v4u*)d_in[1]; P.p[1] = (const v4u*)d_in[2]; P.p[2] = (const v4u*)d_in[3]; P.p[3] = (const v4u*)d_in[4]; P.p[4] = (const v4u*)d_in[8]; P.p[5] = (const v4u*)d_in[9]; int np = 6;
    u64* stamps = (u64*)((char*)d_ws + ((size_t)64 << 20));
    kstream<0><<<256, 512, 0, stream>>>(P, np, stamps);
    kdelay<<<1, 1, 0, stream>>>(stamps, 100);
    u16* ws = (u16*)d_ws;
    u16* out = (u16*)d_out;
    for (int br = 0; br < 2; ++br) {
        const u16* x = (const u16*)d_in[br * 5 + 0];
        const u16* kc = (const u16*)d_in[br * 5 + 1];
        const u16* vc = (const u16*)d_in[br * 5 + 2];
        const u16* wq = (const u16*)d_in[br * 5 + 3];
        const u16* wo = (const u16*)d_in[br * 5 + 4];
        u16* q = ws + (size_t)br * 2 * 512 * 4096;
        u16* o = q + (size_t)512 * 4096;
        gemm_naive<<<dim3(16, 512), 256, 0, stream>>>(x, wq, q, 512, 4096, 1024, 4096);
        attn_naive<<<dim3(32, 32, 16), 256, 0, stream>>>(q, kc, vc, o, br == 0 ? 4 : 32);
        gemm_naive<<<dim3(4, 512), 256, 0, stream>>>(o, wo, out + br * 1024, 512, 1024, 4096, 2048);
    }
}
```

```cpp
#include <hip/hip_runtime.h>
#include <stdint.h>

typedef unsigned short u16;
typedef unsigned int u32;
typedef unsigned long long u64;
typedef short s16x4 __attribute__((ext_vector_type(4)));
typedef short bf16x8 __attribute__((ext_vector_type(8)));
typedef float f32x16 __attribute__((ext_vector_type(16)));
typedef float f32x4 __attribute__((ext_vector_type(4)));
typedef float f32x2 __attribute__((ext_vector_type(2)));
typedef __bf16 bfv2 __attribute__((ext_vector_type(2)));
typedef u32 u32x4 __attribute__((ext_vector_type(4)));
typedef u32 u32x2 __attribute__((ext_vector_type(2)));
#define LAS __attribute__((address_space(3)))
#define GAS __attribute__((address_space(1)))
typedef GAS u64 gu64;
#define DI __device__ __forceinline__
#define MFMA(a, b, c) __builtin_amdgcn_mfma_f32_32x32x16_bf16((a), (b), (c), 0, 0, 0)
#define SB() __builtin_amdgcn_sched_barrier(0)
#define RLX_AGENT __ATOMIC_RELAXED, __HIP_MEMORY_SCOPE_AGENT

constexpr int AUX_DEF = 0, AUX_NT = 2, AUX_SC1 = 16;

DI u32x4 mk_srd(const void* p) {
    uint64_t a = (uint64_t)p;
    u32x4 r; r.x = (u32)a; r.y = (u32)(a >> 32) & 0xffffu; r.z = 0x7fffffffu; r.w = 0x00020000u; return r;
}
DI __amdgpu_buffer_rsrc_t mk_rs(const void* p) { return __builtin_amdgcn_make_buffer_rsrc((void*)p, 0, 0x7fffffff, 0x00020000); }

#define DMA4_ASM(POL) asm volatile("s_mov_b32 m0, %0\n\ts_nop 3\n\tbuffer_load_dwordx4 %1, %5, %6 offen" POL " lds\n\tbuffer_load_dwordx4 %2, %5, %6 offen offset:1024" POL " lds\n\t" \
                     "buffer_load_dwordx4 %3, %5, %6 offen offset:2048" POL " lds\n\tbuffer_load_dwordx4 %4, %5, %6 offen offset:3072" POL " lds" \
                     :: "s"(lds), "v"(v0), "v"(v1), "v"(v2), "v"(v3), "s"(srd), "s"(soff) : "memory")
#define RFL(x) __builtin_amdgcn_readfirstlane(x)
#define UNI() do { soff = RFL(soff); lds = RFL(lds); srd.x = RFL(srd.x); srd.y = RFL(srd.y); } while (0)
template <int AUX> DI void dma4(u32x4 srd, u32 v0, u32 v1, u32 v2, u32 v3, u32 soff, u32 lds) {
    UNI();
    if constexpr (AUX == AUX_NT) DMA4_ASM(" nt"); else if constexpr (AUX == AUX_SC1) DMA4_ASM(" sc1"); else DMA4_ASM("");
}
#define DMA2_ASM(POL) asm volatile("s_mov_b32 m0, %0\n\ts_nop 3\n\tbuffer_load_dwordx4 %1, %3, %4 offen" POL " lds\n\tbuffer_load_dwordx4 %2, %3, %4 offen offset:1024" POL " lds" \
                     :: "s"(lds), "v"(v0), "v"(v1), "s"(srd), "s"(soff) : "memory")
template <int AUX> DI void dma2(u32x4 srd, u32 v0, u32 v1, u32 soff, u32 lds) {
    UNI();
    if constexpr (AUX == AUX_NT) DMA2_ASM(" nt"); else if constexpr (AUX == AUX_SC1) DMA2_ASM(" sc1"); else DMA2_ASM("");
}
template <int N> DI void wait_vm() { asm volatile("s_waitcnt vmcnt(%0)" :: "i"(N) : "memory"); }
DI void wait_lgkm() { asm volatile("s_waitcnt lgkmcnt(0)" ::: "memory"); }
DI void bar() { asm volatile("s_waitcnt lgkmcnt(0)" ::: "memory"); __builtin_amdgcn_s_barrier(); asm volatile("" ::: "memory"); }

DI bf16x8 lds_b128(u32 addr) { return *(LAS bf16x8*)(uintptr_t)addr; }
DI bf16x8 lds_tr2(u32 a0, u32 a1) {
    s16x4 lo = __builtin_amdgcn_ds_read_tr16_b64_v4i16((LAS s16x4*)(uintptr_t)a0);
    s16x4 hi = __builtin_amdgcn_ds_read_tr16_b64_v4i16((LAS s16x4*)(uintptr_t)a1);
    return __builtin_shufflevector(lo, hi, 0, 1, 2, 3, 4, 5, 6, 7);
}
DI u32 pk2(float a, float b) { f32x2 v = {a, b}; return __builtin_bit_cast(u32, __builtin_convertvector(v, bfv2)); }
DI u32x4 pack8(const f32x16& x, int t) {
    u32x4 r; r.x = pk2(x[8 * t], x[8 * t + 1]); r.y = pk2(x[8 * t + 2], x[8 * t + 3]); r.z = pk2(x[8 * t + 4], x[8 * t + 5]); r.w = pk2(x[8 * t + 6], x[8 * t + 7]); return r;
}
DI float swap32_max(float x) {
    auto r = __builtin_amdgcn_permlane32_swap(__float_as_uint(x), __float_as_uint(x), false, false);
    return fmaxf(__uint_as_float(r[0]), __uint_as_float(r[1]));
}
DI float swap32_sum(float x) {
    auto r = __builtin_amdgcn_permlane32_swap(__float_as_uint(x), __float_as_uint(x), false, false);
    return __uint_as_float(r[0]) + __uint_as_float(r[1]);
}

struct Params {
    const u16* x[2]; const u16* k[2]; const u16* v[2]; const u16* wq[2]; const u16* wo[2];
    u16* out; char* ws;
};
constexpr size_t WS_FLAGS = 0;
constexpr size_t WS_OT = 1 << 20, OT_BYTES = (size_t)2 * 512 * 8192;
constexpr size_t WS_PART = WS_OT + OT_BYTES, PART_BYTES = (size_t)256 * 65536;
constexpr size_t WS_QH = WS_PART + PART_BYTES;
constexpr int F_EPOCH = 0, F_O = 64, F_P = 1024, F_OB = 2048, F_Q = 3072;
constexpr float SL2E = 0.08838834764831845f * 1.4426950408889634f;
constexpr u32 SPIN_CAP = 1u << 16;
constexpr int POLL_SLEEP = 2;
constexpr int NPRE = 6, NPRE0 = 4;
constexpr int FS = 16;

struct Ctx { u32 L; int lane, w; u64 tag; gu64* fl; };

DI int olane(const Ctx& C) { int l = C.lane; asm volatile("" : "+v"(l)); return l; }
DI void poll_grid(const Ctx& C, int word, int rows, int cols, int lane) {
    gu64* p = C.fl + (size_t)(word + ((lane >> 3) & (rows - 1)) * 32 + (lane & (cols - 1))) * FS;
    for (u32 spins = 0; spins < SPIN_CAP; ++spins) {
        const u64 v = __hip_atomic_load(p, RLX_AGENT);
        if (__all(v == C.tag)) break;
        __builtin_amdgcn_s_sleep(POLL_SLEEP);
    }
    __builtin_amdgcn_fence(__ATOMIC_ACQUIRE, "wavefront");
}
DI void publish(const Ctx& C, int word, int n) {
    wait_vm<0>();
    bar();
    if (C.w == 0 && C.lane < n) __hip_atomic_store(C.fl + (size_t)(word + C.lane) * FS, C.tag, RLX_AGENT);
}

constexpr u32 SLOT = 32768, SCR = 131072;
constexpr int LDS1_BYTES = 160 * 1024;
DI void rawbar() { __builtin_amdgcn_s_barrier(); asm volatile("" ::: "memory"); }

struct Job { int bB, hB, rotB, bA, hA, rotA, rotK; };

struct LdQB { u32x4 W, X; u32 wv0, wv1, wv2, wv3, av0, av1, av2, av3; int rot, lw; u32 L;
    DI void init(const Params& P, const Job& J, int lane, int lw_, u32 L_) { lw = lw_; L = L_; rot = J.rotB;
        W = mk_srd(P.wq[1] + J.hB * 128); X = mk_srd(P.x[1] + (size_t)J.bB * 32 * 1024);
        wv0 = (lane >> 4) * 8192 + 16 * ((lane & 15) ^ ((lane >> 4) << 2)); wv1 = wv0 + 4 * 8192 - 1024; wv2 = wv0 + 8 * 8192 - 2048; wv3 = wv0 + 12 * 8192 - 3072;
        av0 = (lane >> 3) * 2048 + 16 * ((lane & 7) ^ (lane >> 4)); av1 = (av0 ^ 64) + 8 * 2048 - 1024; av2 = av0 + 16 * 2048 - 2048; av3 = av1 + 16 * 2048 - 2048; }
    DI void issue(int st) const { const int ks = (st & 12) | ((st + (rot >> 1)) & 3); const u32 d = L + (st & 3) * SLOT + lw * 4096;
        dma4<AUX_DEF>(W, wv0, wv1, wv2, wv3, (u32)(ks * 64 + lw * 16) * 8192, d);
        dma4<AUX_DEF>(X, av0, av1, av2, av3, (u32)(lw * 32 * 2048 + ks * 128), d + 16384); }
    static constexpr int CNT = 8, NST = 16; };
struct LdQA { u32x4 W, X; u32 wv0, wv1, wv2, wv3, av0, av1; int rot, lw; u32 L;
    DI void init(const Params& P, const Job& J, int lane, int lw_, u32 L_) { lw = lw_; L = L_; rot = J.rotA;
        W = mk_srd(P.wq[0] + J.hA * 128); X = mk_srd(P.x[0] + (size_t)J.bA * 32 * 1024);
        wv0 = (lane >> 4) * 8192 + 16 * ((lane & 15) ^ ((lane >> 4) << 2)); wv1 = wv0 + 4 * 8192 - 1024; wv2 = wv0 + 8 * 8192 - 2048; wv3 = wv0 + 12 * 8192 - 3072;
        av0 = (lane >> 3) * 2048 + 16 * ((lane & 7) ^ (lane >> 4)); av1 = (av0 ^ 64) + 8 * 2048 - 1024; }
    DI void issue(int st) const { const int ks = (st & 12) | ((st + (rot >> 1)) & 3); const u32 d = L + (st & 3) * SLOT + lw * 4096;
        dma2<AUX_DEF>(X, av0, av1, (u32)(lw * 16 * 2048 + ks * 128), L + SCR + (st & 3) * 8192 + lw * 2048);
        dma4<AUX_DEF>(W, wv0, wv1, wv2, wv3, (u32)(ks * 64 + lw * 16) * 8192, d);
        dma4<AUX_DEF>(W, wv0, wv1, wv2, wv3, (u32)(ks * 64 + lw * 16) * 8192 + 256, d + 16384); }
    static constexpr int CNT = 10, NST = 16; };
struct LdAT { u32x4 K, V; u32 kv0, kv1, kv2, kv3, vv; int rot, lw; u32 L;
    DI void init(const Params& P, const Job& J, int lane, int lw_, u32 L_) { lw = lw_; L = L_; rot = J.rotK;
        const size_t kvo = (size_t)(J.bA * 4 + (J.hA >> 3)) * 65536; K = mk_srd(P.k[0] + kvo); V = mk_srd(P.v[0] + kvo);
        kv0 = (lane >> 4) * 256 + 16 * ((lane & 15) ^ (lane >> 4)); kv1 = kv0 ^ 64; kv2 = kv0 ^ 128; kv3 = kv0 ^ 192;
        vv = (lane >> 4) * 256 + 16 * ((lane & 15) ^ ((lane >> 4) << 2)); }
    DI void issue(int st) const {
        const int pr = (st >> 3) * 4 + ((((st >> 2) & 1) * 2 + (st & 1) + rot) & 3);
        const u32 so = (lw & 1) * 524288 + pr * 16384 + (lw >> 1) * 8192, d = L + (st & 3) * SLOT + lw * 8192;
        if ((st & 2) == 0) { dma4<AUX_DEF>(K, kv0, kv1, kv2, kv3, so, d); dma4<AUX_DEF>(K, kv0, kv1, kv2, kv3, so + 4096, d + 4096); }
        else { dma4<AUX_DEF>(V, vv, vv, vv, vv, so, d); dma4<AUX_DEF>(V, vv, vv, vv, vv, so + 4096, d + 4096); } }
    static constexpr int CNT = 8, NST = 16; };
struct LdB { u32x4 K, V; u32 kv0, kv1, kv2, kv3, vv; int lw; u32 L;
    DI void init(const Params& P, const Job& J, int lane, int lw_, u32 L_, int rnd) { lw = lw_; L = L_;
        const size_t kvo = (size_t)((J.bB + 2 * rnd + (lw >> 1)) * 32 + J.hB) * 65536; K = mk_srd(P.k[1] + kvo); V = mk_srd(P.v[1] + kvo);
        kv0 = (lane >> 4) * 256 + 16 * ((lane & 15) ^ (lane >> 4)); kv1 = kv0 ^ 64; kv2 = kv0 ^ 128; kv3 = kv0 ^ 192;
        vv = (lane >> 4) * 256 + 16 * ((lane & 15) ^ ((lane >> 4) << 2)); }
    DI void issue(int st) const { const int T = (st >> 3) * 4 + (st & 3);
        const u32 so = (2 * T + (lw & 1)) * 8192, d = L + (st & 3) * SLOT + lw * 8192;
        if ((st & 4) == 0) { dma4<AUX_NT>(K, kv0, kv1, kv2, kv3, so, d); dma4<AUX_NT>(K, kv0, kv1, kv2, kv3, so + 4096, d + 4096); }
        else { dma4<AUX_NT>(V, vv, vv, vv, vv, so, d); dma4<AUX_NT>(V, vv, vv, vv, vv, so + 4096, d + 4096); } }
    static constexpr int CNT = 8, NST = 16; };
struct LdB1 { u32x4 K, V; u32 kv0, kv1, kv2, kv3, vv; int lw; u32 L;
    DI void init(const Params& P, int b, int h, int lane, int lw_, u32 L_) { lw = lw_; L = L_;
        const size_t kvo = (size_t)(b * 32 + h) * 65536; K = mk_srd(P.k[1] + kvo); V = mk_srd(P.v[1] + kvo);
        kv0 = (lane >> 4) * 256 + 16 * ((lane & 15) ^ (lane >> 4)); kv1 = kv0 ^ 64; kv2 = kv0 ^ 128; kv3 = kv0 ^ 192;
        vv = (lane >> 4) * 256 + 16 * ((lane & 15) ^ ((lane >> 4) << 2)); }
    DI void issue(int st) const {
        const u32 so = (4 * (st & 3) + lw) * 8192, d = L + (st & 3) * SLOT + lw * 8192;
        if ((st & 4) == 0) { dma4<AUX_NT>(K, kv0, kv1, kv2, kv3, so, d); dma4<AUX_NT>(K, kv0, kv1, kv2, kv3, so + 4096, d + 4096); }
        else { dma4<AUX_NT>(V, vv, vv, vv, vv, so, d); dma4<AUX_NT>(V, vv, vv, vv, vv, so + 4096, d + 4096); } }
    static constexpr int CNT = 8, NST = 8; };
struct LdOA { u32x4 W, X; u32 wv0, wv1, wv2, wv3, av0; int rot, lw; u32 L;
    DI void init(const Params& P, int br, int ksl, int mt, int nt, int rot_, int lane, int lw_, u32 L_) { lw = lw_; L = L_; rot = rot_;
        W = mk_srd((br ? P.wo[1] : P.wo[0]) + (size_t)ksl * 1024 * 1024 + nt * 128); X = mk_srd(P.ws + WS_OT + ((size_t)(br * 16 + mt * 4) * 256 + ksl * 64) * 1024);
        wv0 = (lane >> 4) * 2048 + 16 * ((lane & 15) ^ ((lane >> 4) << 2)); wv1 = wv0 + 4 * 2048 - 1024; wv2 = wv0 + 8 * 2048 - 2048; wv3 = wv0 + 12 * 2048 - 3072; av0 = lane * 16; }
    DI void issueW(int st) const { const int ks = (st & 8) | ((st + rot) & 7); dma4<AUX_DEF>(W, wv0, wv1, wv2, wv3, (u32)(ks * 64 + lw * 16) * 2048, L + (st & 3) * SLOT + lw * 4096); }
    DI void issueX(int st) const { const int ks = (st & 8) | ((st + rot) & 7); dma4<AUX_SC1>(X, av0, av0, av0, av0, (u32)(lw * 262144 + ks * 4096), L + (st & 3) * SLOT + 16384 + lw * 4096); }
    DI void issue(int st) const { issueW(st); issueX(st); }
    static constexpr int CNT = 8, NST = 16; };
template <class A, class B, int XB> DI void loader_phase(const Ctx& C, const A& a, const B& b) {
#pragma unroll 1
    for (int st = 0; st < A::NST - 3; ++st) { wait_vm<2 * A::CNT>(); rawbar(); a.issue(st + 3); }
    wait_vm<2 * A::CNT>(); rawbar(); b.issue(0);
    wait_vm<A::CNT + B::CNT>(); rawbar(); b.issue(1);
    wait_vm<2 * B::CNT>(); rawbar(); b.issue(2);
#pragma unroll
    for (int i = 0; i < XB; ++i) { rawbar(); }
}

struct GFrag { bf16x8 a[2][2], b[2][2]; };
template <int MODE> DI void gemm128(const Ctx& C, f32x16 (&acc)[2][2], int pend) {
    const int lane = olane(C), w = C.w, wn = w & 1, wm = w >> 1;
    const int hh = lane >> 5, q = (lane & 15) >> 2, blk = (lane >> 4) & 1, p = lane & 3, pp = ((p & 1) << 1) | (p >> 1), r = lane & 31;
    u32 tb[2], ab[4];
#pragma unroll
    for (int nb = 0; nb < 2; ++nb) tb[nb] = C.L + 256 * (8 * hh + q) + 64 * ((2 * wn + nb) ^ q) + 32 * blk + 8 * pp;
#pragma unroll
    for (int s = 0; s < 4; ++s) ab[s] = MODE == 0 ? C.L + 16384 + 128 * (64 * wm + r) + 16 * ((2 * s + hh) ^ ((r >> 1) & 7)) : C.L + 16384 + wm * 8192 + lane * 16;
    auto up = [&](int d) {
#pragma unroll
        for (int i = 0; i < 2; ++i) { tb[i] += d; asm volatile("" : "+v"(tb[i])); }
#pragma unroll
        for (int i = 0; i < (MODE ? 1 : 4); ++i) { ab[i] += d; asm volatile("" : "+v"(ab[i])); } };
    GFrag F0 = {}, F1 = {};
    auto mm1 = [&](const GFrag& F, int i) { const int s = i >> 2, nb = (i >> 1) & 1, mb = i & 1; acc[nb][mb] = MFMA(F.a[s][nb], F.b[s][mb], acc[nb][mb]); };
    auto rd1 = [&](GFrag& F, int sl, int hf, int i) { const int s = i >> 1, x = i & 1, k = 2 * hf + s; const u32 so = (sl & 1) * SLOT;
        F.a[s][x] = lds_tr2(tb[x] + so + 4096 * k, tb[x] + so + 4096 * k + 1024);
        F.b[s][x] = MODE ? lds_b128(ab[0] + so + x * 4096 + k * 1024) : lds_b128(ab[k] + so + x * 4096); };
#define QH(SL, HF, FC, FP, POST) do { SB(); mm1(FP, 0); SB(); rd1(FC, SL, HF, 0); SB(); mm1(FP, 1); SB(); rd1(FC, SL, HF, 1); SB(); mm1(FP, 2); SB(); rd1(FC, SL, HF, 2); SB(); mm1(FP, 3); SB(); rd1(FC, SL, HF, 3); SB(); \
        mm1(FP, 4); SB(); POST; SB(); mm1(FP, 5); mm1(FP, 6); mm1(FP, 7); SB(); } while (0)
#pragma unroll 1
    for (int st = 0; st < 16; st += 2) {
        if (MODE == 1 && st == 0 && pend >= 0) wait_vm<0>();
        bar();
        if (MODE == 1 && st == 0 && pend >= 0) { if (w == 0 && lane == 0) __hip_atomic_store(C.fl + (size_t)pend * FS, C.tag, RLX_AGENT); }
        QH(0, 0, F0, F1, ); QH(0, 1, F1, F0, );
        bar();
        QH(1, 0, F0, F1, ); QH(1, 1, F1, F0, up((st & 2) ? -65536 : 65536));
    }
#undef QH
#pragma unroll
    for (int i = 0; i < 8; ++i) mm1(F1, i);
}
DI void comp_qb(const Ctx& C, const Params& P) {
    const u32 L = C.L; const int lane = olane(C), w = C.w, wn = w & 1, wm = w >> 1;
    f32x16 acc[2][2] = {};
    gemm128<0>(C, acc, -1);
#pragma unroll
    for (int nb = 0; nb < 2; ++nb)
#pragma unroll
        for (int mb = 0; mb < 2; ++mb)
#pragma unroll
            for (int t = 0; t < 2; ++t) *(LAS u32x4*)(uintptr_t)(L + SCR + (2 * wm + mb) * 8192 + (2 * (2 * wn + nb) + t) * 1024 + lane * 16) = pack8(acc[nb][mb], t);
    bar();
}
constexpr size_t PARTA = 0;
DI f32x16 comp_oa(const Ctx& C, const Params& P, int br, int ksl, int mt, int nt, int pend) {
    const int lane = olane(C), w = C.w;
    f32x16 acc[2][2] = {};
    gemm128<1>(C, acc, pend);
    __amdgpu_buffer_rsrc_t rp = mk_rs(P.ws + WS_PART + PARTA);
    const int tile = (br * 4 + mt) * 8 + nt;
    f32x16 own;
#pragma unroll
    for (int bq = 0; bq < 4; ++bq) {
        const f32x16& a = acc[bq >> 1][bq & 1];
        if (bq == ksl) own = a;
        else {
            const int off = (((tile * 4 + bq) * 4 + ksl) * 4 + w) * 2048;
#pragma unroll
            for (int t = 0; t < 2; ++t) __builtin_amdgcn_raw_buffer_store_b128(pack8(a, t), rp, lane * 16, off + t * 1024, AUX_SC1);
        }
    }
    return own;
}
DI void final_oa(const Ctx& C, const Params& P, int br, int ksl, int mt, int nt, f32x16 own) {
    const int lane = olane(C), w = C.w, tile = (br * 4 + mt) * 8 + nt;
    if (w < 4) {
        poll_grid(C, F_P + tile * 4, 1, 4, lane);
        __amdgpu_buffer_rsrc_t rp = mk_rs(P.ws + WS_PART + PARTA);
        u32x4 v[3][2];
#pragma unroll
        for (int i = 1; i < 4; ++i)
#pragma unroll
            for (int t = 0; t < 2; ++t) v[i - 1][t] = __builtin_amdgcn_raw_buffer_load_b128(rp, lane * 16, (((tile * 4 + ksl) * 4 + ((ksl + i) & 3)) * 4 + w) * 2048 + t * 1024, AUX_SC1);
#pragma unroll
        for (int i = 0; i < 3; ++i)
#pragma unroll
            for (int t = 0; t < 2; ++t)
#pragma unroll
                for (int e = 0; e < 4; ++e) { own[8 * t + 2 * e] += __uint_as_float(v[i][t][e] << 16); own[8 * t + 2 * e + 1] += __uint_as_float(v[i][t][e] & 0xffff0000u); }
        const int mrow = mt * 128 + (w >> 1) * 64 + (ksl & 1) * 32 + (lane & 31);
#pragma unroll
        for (int t = 0; t < 2; ++t) *(u32x4*)(P.out + (size_t)mrow * 2048 + br * 1024 + nt * 128 + (w & 1) * 64 + (ksl >> 1) * 32 + 16 * t + 8 * (lane >> 5)) = pack8(own, t);
    }
}
DI void comp_qa(const Ctx& C, const Params& P, bf16x8 (&qf)[8]) {
    const u32 L = C.L; const int lane = olane(C), w = C.w, bi = w >> 1, hi_ = w & 1;
    const int hh = lane >> 5, q = (lane & 15) >> 2, blk = (lane >> 4) & 1, p = lane & 3, pp = ((p & 1) << 1) | (p >> 1), r = lane & 31;
    u32 tb[4], ab[4];
#pragma unroll
    for (int nb = 0; nb < 4; ++nb) tb[nb] = L + hi_ * 16384 + 256 * (8 * hh + q) + 64 * (nb ^ q) + 32 * blk + 8 * pp;
#pragma unroll
    for (int s = 0; s < 4; ++s) ab[s] = L + SCR + 128 * (32 * bi + r) + 16 * ((2 * s + hh) ^ ((r >> 1) & 7));
    auto up = [&](int d) {
#pragma unroll
        for (int i = 0; i < 4; ++i) { tb[i] += d; ab[i] += d >> 2; asm volatile("" : "+v"(tb[i]), "+v"(ab[i])); } };
    struct Fr { bf16x8 a[2][4], b[2]; };
    f32x16 acc[4] = {};
    Fr F0 = {}, F1 = {};
    auto mm1 = [&](const Fr& F, int i) { acc[i & 3] = MFMA(F.a[i >> 2][i & 3], F.b[i >> 2], acc[i & 3]); };
    auto rd1 = [&](Fr& F, int sl, int hf, int i) { const u32 so = (sl & 1) * SLOT; const int s = i >> 1, n2 = (i & 1) * 2, k = 2 * hf + s;
        if (n2 == 0) F.b[s] = lds_b128(ab[k] + (sl & 1) * 8192);
        F.a[s][n2] = lds_tr2(tb[n2] + so + 4096 * k, tb[n2] + so + 4096 * k + 1024); F.a[s][n2 + 1] = lds_tr2(tb[n2 + 1] + so + 4096 * k, tb[n2 + 1] + so + 4096 * k + 1024); };
#define QH(SL, HF, FC, FP, POST) do { SB(); mm1(FP, 0); SB(); rd1(FC, SL, HF, 0); SB(); mm1(FP, 1); SB(); rd1(FC, SL, HF, 1); SB(); mm1(FP, 2); SB(); rd1(FC, SL, HF, 2); SB(); mm1(FP, 3); SB(); rd1(FC, SL, HF, 3); SB(); \
        mm1(FP, 4); SB(); POST; SB(); mm1(FP, 5); mm1(FP, 6); mm1(FP, 7); SB(); } while (0)
#pragma unroll 1
    for (int st = 0; st < 16; st += 2) {
        bar(); QH(0, 0, F0, F1, ); QH(0, 1, F1, F0, );
        bar(); QH(1, 0, F0, F1, ); QH(1, 1, F1, F0, up((st & 2) ? -65536 : 65536));
    }
#undef QH
#pragma unroll
    for (int i = 0; i < 8; ++i) mm1(F1, i);
#pragma unroll
    for (int nb = 0; nb < 4; ++nb)
#pragma unroll
        for (int t = 0; t < 2; ++t) qf[2 * nb + t] = __builtin_bit_cast(bf16x8, pack8(acc[nb], t));
}
template <int NCH> DI void comp_att(const Ctx& C, u32 toff, const bf16x8 (&qf)[8], float& m, float& l, f32x16 (&O)[4], int pend, int pendn, int aflag) {
    const int lane = olane(C);
    const int h = lane >> 5, q = (lane & 15) >> 2, blk = (lane >> 4) & 1, p = lane & 3, pp = ((p & 1) << 1) | (p >> 1), r = lane & 31;
    u32 kb[8], vb[4];
#pragma unroll
    for (int s = 0; s < 8; ++s) kb[s] = C.L + toff + 256 * r + 16 * ((2 * s + h) ^ (r & 15));
#pragma unroll
    for (int c = 0; c < 4; ++c) { vb[c] = C.L + toff + (NCH == 4 ? 65536 : 0) + 256 * (4 * h + q) + 64 * (c ^ q) + 32 * blk + 8 * pp; asm volatile("" : "+v"(vb[c])); }
    bf16x8 F0[8], F1[8] = {};
    u32x4 pf[4][2] = {};
    float mx = 0.f, nm = 0.f, ls = 0.f;
    f32x16 S[4];
    auto kup = [&](int d) { if (NCH == 4) return;
#pragma unroll
        for (int s = 0; s < 8; ++s) { kb[s] += d; asm volatile("" : "+v"(kb[s])); } };
    auto vup = [&](int d) { if (NCH == 4) return;
#pragma unroll
        for (int c = 0; c < 4; ++c) { vb[c] += d; asm volatile("" : "+v"(vb[c])); } };
    auto tof = [&](int t) -> u32 { return NCH == 4 ? t * 16384 : (t & 1) * SLOT; };
    auto rdk = [&](bf16x8 (&F)[8], int t, int i) { F[2 * i] = lds_b128(kb[2 * i] + tof(t)); F[2 * i + 1] = lds_b128(kb[2 * i + 1] + tof(t)); };
    auto rdv = [&](bf16x8 (&F)[8], int t, int c) { const u32 base = vb[c] + tof(t); F[2 * c] = lds_tr2(base, base + 2048); F[2 * c + 1] = lds_tr2(base + 4096, base + 6144); };
    auto mk = [&](const bf16x8 (&F)[8], int t, int i) { if (i == 0) S[t] = MFMA(F[0], qf[0], f32x16{}); else S[t] = MFMA(F[i], qf[i], S[t]); };
    auto mv = [&](const bf16x8 (&F)[8], int t, int i) { O[i >> 1] = MFMA(F[i], __builtin_bit_cast(bf16x8, pf[t][i & 1]), O[i >> 1]); };
    auto fnone = [&](int, int) {};
    auto fmax2 = [&](int t, int i) { mx = (t == 0 && i == 0) ? fmaxf(S[0][0], S[0][1]) : fmaxf(fmaxf(mx, S[t][2 * i]), S[t][2 * i + 1]); asm volatile("" : "+v"(mx)); };
    auto fexp2 = [&](int t, int i) { const float e0 = __builtin_amdgcn_exp2f(fmaf(S[t][2 * i], SL2E, nm)), e1 = __builtin_amdgcn_exp2f(fmaf(S[t][2 * i + 1], SL2E, nm));
        ls += e0; ls += e1; pf[t][i >> 2][i & 3] = pk2(e0, e1); };
#define AST(RD, FC, TC, MM, FP, TP, POST, FL, TF) do { if (NCH != 4 || ((TC) & 1) == 0) bar(); SB(); \
        MM(FP, TP, 0); SB(); RD(FC, TC, 0); FL(TF, 0); SB(); MM(FP, TP, 1); SB(); RD(FC, TC, 1); FL(TF, 1); SB(); MM(FP, TP, 2); SB(); RD(FC, TC, 2); FL(TF, 2); SB(); MM(FP, TP, 3); SB(); RD(FC, TC, 3); FL(TF, 3); SB(); \
        MM(FP, TP, 4); SB(); POST; FL(TF, 4); SB(); MM(FP, TP, 5); SB(); FL(TF, 5); SB(); MM(FP, TP, 6); SB(); FL(TF, 6); SB(); MM(FP, TP, 7); SB(); FL(TF, 7); SB(); } while (0)
#pragma unroll 1
    for (int ch = 0; ch < NCH; ++ch) {
        AST(rdk, F0, 0, mv, F1, 3, , fnone, 0);
        AST(rdk, F1, 1, mk, F0, 0, kup(65536), fnone, 0);
        if (NCH != 4 && pend >= 0) wait_vm<0>();
        AST(rdk, F0, 2, mk, F1, 1, , fmax2, 0);
        if (NCH != 4 && pend >= 0) { if (C.w == 0 && lane < (pendn > 0 ? pendn : -pendn)) __hip_atomic_store(C.fl + (size_t)(pend + (pendn > 0 ? 32 : 1) * lane) * FS, C.tag, RLX_AGENT); pend = -1; }
        if (NCH == 4 && ch == 2 && C.w == 0) poll_grid(C, aflag, 1, 1, lane);
        AST(rdk, F1, 3, mk, F0, 2, kup(-65536), fmax2, 1);
        AST(rdv, F0, 0, mk, F1, 3, , fmax2, 2);
        {
#pragma unroll
            for (int i = 0; i < 8; ++i) fmax2(3, i);
            float mn = fmaxf(m, swap32_max(mx));
            if (NCH == 1) {
                LAS float* st = (LAS float*)(uintptr_t)(C.L + SCR + 16384);
                if (lane < 32) st[C.w * 32 + lane] = mn;
                if (C.w == 0 && lane == 0) *(LAS u64*)(uintptr_t)(C.L + SCR + 16384 + 1024) = C.tag;
                bar();
                mn = fmaxf(fmaxf(st[r], st[32 + r]), fmaxf(st[64 + r], st[96 + r]));
            }
            const float alpha = __builtin_amdgcn_exp2f((m - mn) * SL2E);
            m = mn; nm = -mn * SL2E; ls = l * alpha;
#pragma unroll
            for (int i = 0; i < 8; ++i) fexp2(0, i);
            if (NCH > 1) {
#pragma unroll
                for (int c = 0; c < 4; ++c)
#pragma unroll
                    for (int i = 0; i < 16; ++i) O[c][i] *= alpha;
            }
        }
        AST(rdv, F1, 1, mv, F0, 0, vup(65536), fexp2, 1);
        AST(rdv, F0, 2, mv, F1, 1, , fexp2, 2);
        AST(rdv, F1, 3, mv, F0, 2, vup(-65536), fexp2, 3);
        l = ls;
    }
#undef AST
#pragma unroll
    for (int i = 0; i < 8; ++i) mv(F1, 3, i);
}
DI void comp_a(const Ctx& C, const Params& P, const Job& J) {
    bf16x8 qf[8];
    comp_qa(C, P, qf);
    float m = -3.0e38f, l = 0.f;
    f32x16 O[4] = {};
    comp_att<4>(C, (C.w >> 1) * 8192, qf, m, l, O, -1, 0, F_Q + (J.bB >> 2) * 32 + J.hB);
    l = swap32_sum(l);
    const float inv = 1.0f / l;
    __amdgpu_buffer_rsrc_t ro = mk_rs(P.ws + WS_OT);
#pragma unroll
    for (int c = 0; c < 4; ++c) {
#pragma unroll
        for (int i = 0; i < 16; ++i) O[c][i] *= inv;
#pragma unroll
        for (int t = 0; t < 2; ++t) __builtin_amdgcn_raw_buffer_store_b128(pack8(O[c], t), ro, olane(C) * 16, (((J.bA + (C.w >> 1)) * 32 + J.hA + (C.w & 1)) * 8 + 2 * c + t) * 1024, AUX_SC1);
    }
}
DI void comp_b1(const Ctx& C, const Params& P, int b, int h, const bf16x8 (&qf)[8], int pend, int pendn) {
    const u32 L = C.L; const int w = C.w;
    float m = -3.0e38f, l = 0.f;
    f32x16 O[4] = {};
    comp_att<1>(C, w * 8192, qf, m, l, O, pend, pendn, -1);
    l = swap32_sum(l);
    const int lane = olane(C);
    LAS float* st = (LAS float*)(uintptr_t)(L + SCR + 16384 + 512);
    if (lane < 32) st[w * 32 + lane] = l;
    wait_lgkm();
    f32x16 own;
#pragma unroll
    for (int c = 0; c < 4; ++c) {
        const int d = (c - w) & 3;
        if (d == 0) own = O[c];
        else {
            const u32 dst = (d == 3 ? L + SCR + w * 4096 : L + 3 * SLOT + w * 8192 + (d - 1) * 4096) + lane * 16;
#pragma unroll
            for (int g4 = 0; g4 < 4; ++g4) { f32x4 v = {O[c][4 * g4], O[c][4 * g4 + 1], O[c][4 * g4 + 2], O[c][4 * g4 + 3]}; *(LAS f32x4*)(uintptr_t)(dst + g4 * 1024) = v; }
        }
    }
    bar();
    const float inv = 1.0f / ((st[lane & 31] + st[32 + (lane & 31)]) + (st[64 + (lane & 31)] + st[96 + (lane & 31)]));
#pragma unroll
    for (int d = 1; d < 4; ++d) {
        const int src = (w - d) & 3;
        const u32 a = (d == 3 ? L + SCR + src * 4096 : L + 3 * SLOT + src * 8192 + (d - 1) * 4096) + lane * 16;
#pragma unroll
        for (int g4 = 0; g4 < 4; ++g4) { f32x4 v = *(LAS f32x4*)(uintptr_t)(a + g4 * 1024);
#pragma unroll
            for (int e = 0; e < 4; ++e) own[4 * g4 + e] += v[e]; }
    }
#pragma unroll
    for (int i = 0; i < 16; ++i) own[i] *= inv;
    __amdgpu_buffer_rsrc_t ro = mk_rs(P.ws + WS_OT);
#pragma unroll
    for (int t = 0; t < 2; ++t) __builtin_amdgcn_raw_buffer_store_b128(pack8(own, t), ro, lane * 16, (((16 + b) * 32 + h) * 8 + 2 * w + t) * 1024, AUX_SC1);
}
DI void comp_b(const Ctx& C, const Params& P, const Job& J) {
    const u32 L = C.L; const int w = C.w, kh = w & 1;
    comp_qb(C, P);
    {
        bf16x8 qf[8];
        { const int lane = olane(C);
            __amdgpu_buffer_rsrc_t rq = mk_rs(P.ws + WS_QH);
#pragma unroll
            for (int t = 0; t < 2; ++t) __builtin_amdgcn_raw_buffer_store_b128(__builtin_bit_cast(u32x4, lds_b128(L + SCR + 3 * 8192 + (2 * w + t) * 1024 + lane * 16)), rq, lane * 16,
                                                                                (((J.bB >> 2) * 32 + J.hB) * 8 + 2 * w + t) * 1024, AUX_SC1);
#pragma unroll
            for (int s = 0; s < 8; ++s) qf[s] = lds_b128(L + SCR + (w >> 1) * 8192 + s * 1024 + lane * 16); }
        float m = -3.0e38f, l = 0.f;
        f32x16 O[4] = {};
        comp_att<2>(C, w * 8192, qf, m, l, O, F_Q + (J.bB >> 2) * 32 + J.hB, 1, -1);
        l = swap32_sum(l);
        const int lane = olane(C);
        f32x16 snd[2], kp[2];
        if (kh) { snd[0] = O[0]; snd[1] = O[1]; kp[0] = O[2]; kp[1] = O[3]; } else { snd[0] = O[2]; snd[1] = O[3]; kp[0] = O[0]; kp[1] = O[1]; }
        const u32 R = L + 3 * SLOT + w * 8192, R2 = L + 3 * SLOT + (w ^ 1) * 8192;
        {
            LAS float* st = (LAS float*)(uintptr_t)(L + SCR + w * 256);
            wait_lgkm();
            if (lane < 32) { st[lane] = m; st[32 + lane] = l; }
#pragma unroll
            for (int c2 = 0; c2 < 2; ++c2)
#pragma unroll
                for (int g4 = 0; g4 < 4; ++g4) {
                    f32x4 v = {snd[c2][4 * g4], snd[c2][4 * g4 + 1], snd[c2][4 * g4 + 2], snd[c2][4 * g4 + 3]};
                    *(LAS f32x4*)(uintptr_t)(R + c2 * 4096 + g4 * 1024 + lane * 16) = v;
                }
        }
        bar();
        {
            LAS float* st = (LAS float*)(uintptr_t)(L + SCR + (w ^ 1) * 256);
            const float m2 = st[lane & 31], l2 = st[32 + (lane & 31)];
            const float M = fmaxf(m, m2);
            const float e1 = __builtin_amdgcn_exp2f((m - M) * SL2E), e2 = __builtin_amdgcn_exp2f((m2 - M) * SL2E);
            const float inv = 1.0f / (l * e1 + l2 * e2);
            const float s1 = e1 * inv, s2 = e2 * inv;
            __amdgpu_buffer_rsrc_t ro = mk_rs(P.ws + WS_OT);
#pragma unroll
            for (int c2 = 0; c2 < 2; ++c2) {
                f32x16 o;
#pragma unroll
                for (int g4 = 0; g4 < 4; ++g4) {
                    f32x4 v = *(LAS f32x4*)(uintptr_t)(R2 + c2 * 4096 + g4 * 1024 + lane * 16);
#pragma unroll
                    for (int e = 0; e < 4; ++e) o[4 * g4 + e] = kp[c2][4 * g4 + e] * s1 + v[e] * s2;
                }
#pragma unroll
                for (int t = 0; t < 2; ++t) __builtin_amdgcn_raw_buffer_store_b128(pack8(o, t), ro, lane * 16, (((16 + J.bB + (w >> 1)) * 32 + J.hB) * 8 + 2 * (2 * kh + c2) + t) * 1024, AUX_SC1);
            }
        }
    }
    bf16x8 qf[8];
    { const int lane = olane(C);
#pragma unroll
        for (int s = 0; s < 8; ++s) qf[s] = lds_b128(L + SCR + 2 * 8192 + s * 1024 + lane * 16); }
    comp_b1(C, P, J.bB + 2, J.hB, qf, F_OB + J.bB * 32 + J.hB, 2);
}

__global__ __launch_bounds__(512, 2) void k_att(Params P) {
    extern __shared__ __attribute__((aligned(16))) char smem[];
    Ctx C;
    C.L = (u32)(uintptr_t)(LAS char*)smem;
    C.lane = threadIdx.x & 63; C.w = __builtin_amdgcn_readfirstlane(threadIdx.x >> 6); C.tag = 0;
    C.fl = (gu64*)(P.ws + WS_FLAGS);

    if (C.w < 4) {
        const u64 e = __hip_atomic_load(C.fl + F_EPOCH, RLX_AGENT);
        const u32 lo = __builtin_amdgcn_readfirstlane((u32)e), hi = __builtin_amdgcn_readfirstlane((u32)(e >> 32));
        C.tag = (((u64)hi << 32) | lo) * 6364136223846793005ull + 1442695040888963407ull;
    }
    const int c = blockIdx.x, g = c & 7, j = c >> 3, jj = j & 15;
    const bool isx = j < 16;
    Job J;
    J.bB = (jj >> 2) * 4; J.hB = 4 * g + (jj & 3); J.rotB = (((jj >> 2) + (jj & 3)) & 3) * 2;
    J.bA = (g >> 2) * 8 + (jj >> 2) * 2; J.hA = (g & 3) * 8 + (jj & 3) * 2; J.rotA = (((jj >> 2) + (jj & 3)) & 3) * 2; J.rotK = jj & 3;
    const int ksl = g & 3, mq = jj >> 2, obr = g >> 2, mt = j >> 3, nt = j & 7, rot = (2 * mt + nt) & 7;
    f32x16 ownA = {};
    if (C.w < 4) {
        if (isx) {
            comp_a(C, P, J);
            bf16x8 qf[8];
            { const int lane = olane(C);
#pragma unroll
              for (int s = 0; s < 8; ++s) qf[s] = lds_b128(C.L + SCR + 24576 + s * 1024 + lane * 16); }
            comp_b1(C, P, J.bB + 3, J.hB, qf, F_O + (J.bA >> 1) * 32 + J.hA, -2);
        } else comp_b(C, P, J);
        publish(C, F_OB + (J.bB + (isx ? 3 : 2)) * 32 + J.hB, 1);
        ownA = comp_oa(C, P, obr, ksl, mt, nt, -1);
    } else {
        const int lw = C.w - 4;
        if (isx) {
            LdB1 bx; bx.init(P, J.bB + 3, J.hB, C.lane, lw, C.L);
            const size_t kvo = (size_t)((J.bB + 3) * 32 + J.hB) * 65536;
            const __amdgpu_buffer_rsrc_t rK = mk_rs(P.k[1] + kvo), rV = mk_rs(P.v[1] + kvo);
            u32x4 rt[NPRE][8];
#pragma unroll
            for (int st = 0; st < NPRE0; ++st)
#pragma unroll
                for (int i = 0; i < 8; ++i)
                    rt[st][i] = st < 4 ? __builtin_amdgcn_raw_buffer_load_b128(rK, (bx.kv0 ^ (64 * (i & 3))) + (i & 3) * 1024, (4 * st + lw) * 8192 + (i >> 2) * 4096, AUX_NT)
                                       : __builtin_amdgcn_raw_buffer_load_b128(rV, bx.vv + (i & 3) * 1024, (4 * (st - 4) + lw) * 8192 + (i >> 2) * 4096, AUX_NT);
            SB();
            LdQA qa; qa.init(P, J, C.lane, lw, C.L);
            qa.issue(0); qa.issue(1); qa.issue(2);
            LdAT at; at.init(P, J, C.lane, lw, C.L);
            loader_phase<LdQA, LdAT, 0>(C, qa, at);
            const u32 wb = C.L + lw * 8192 + C.lane * 16; u32 wb2 = wb + 65536; asm volatile("" : "+v"(wb2));
            auto str = [&](int st) {
#pragma unroll
                for (int i = 0; i < 8; ++i) *(LAS u32x4*)(uintptr_t)(((st & 2) ? wb2 : wb) + (st & 1) * SLOT + i * 1024) = rt[st % NPRE][i];
                SB();
            };
            auto ldt = [&](int st) {
#pragma unroll
                for (int i = 0; i < 8; ++i)
                    rt[st][i] = st < 4 ? __builtin_amdgcn_raw_buffer_load_b128(rK, (bx.kv0 ^ (64 * (i & 3))) + (i & 3) * 1024, (4 * st + lw) * 8192 + (i >> 2) * 4096, AUX_NT)
                                       : __builtin_amdgcn_raw_buffer_load_b128(rV, bx.vv + (i & 3) * 1024, (4 * (st - 4) + lw) * 8192 + (i >> 2) * 4096, AUX_NT);
                SB(); };
            constexpr int NX = NPRE - NPRE0;
#pragma unroll
            for (int st = 0; st < 13; ++st) {
                const int wprev = (st - 3) >> 2;
                const bool extra = st >= 3 && ((st - 3) & 3) < 3 && wprev >= 3 - NX;
                if (extra) wait_vm<24>(); else wait_vm<16>();
                rawbar();
                if (st < 12) at.issue(st + 3);
                if ((st & 3) == 2 && (st >> 2) >= 3 - NX) ldt(NPRE0 + (st >> 2) - (3 - NX));
            }
            dma2<AUX_SC1>(mk_srd(P.ws + WS_QH + (size_t)(mq * 32 + J.hB) * 8192), C.lane * 16, C.lane * 16, lw * 2048, C.L + SCR + 24576 + lw * 2048);
            at.issue(15);
            auto ldv = [&](int st) {
#pragma unroll
                for (int i = 0; i < 8; ++i) rt[st - 6][i] = __builtin_amdgcn_raw_buffer_load_b128(rV, bx.vv + (i & 3) * 1024, (4 * (st - 4) + lw) * 8192 + (i >> 2) * 4096, AUX_NT);
                SB(); };
            if (NX > 0) wait_vm<26>(); else wait_vm<18>();
            rawbar(); str(0); ldv(6);
            wait_vm<18>(); bar(); str(1); ldv(7);
            wait_vm<16>(); bar(); str(2);
            bar(); str(3);
            bar(); str(4);
            bar(); str(5);
            bar();
            bar(); rawbar(); str(6); str(7);
            LdOA oa; oa.init(P, obr, ksl, mt, nt, rot, C.lane, lw, C.L);
            bar(); oa.issueW(0);
            rawbar(); oa.issueW(1);
            rawbar(); oa.issueW(2);
        } else {
            LdB b0; b0.init(P, J, C.lane, lw, C.L, 0);
            LdQB qb; qb.init(P, J, C.lane, lw, C.L);
            qb.issue(0); qb.issue(1); qb.issue(2);
            loader_phase<LdQB, LdB, 1>(C, qb, b0);
            LdB1 b1; b1.init(P, J.bB + 2, J.hB, C.lane, lw, C.L);
            loader_phase<LdB, LdB1, 1>(C, b0, b1);
            LdOA oa; oa.init(P, obr, ksl, mt, nt, rot, C.lane, lw, C.L);
#pragma unroll 1
            for (int st = 0; st < 5; ++st) { wait_vm<16>(); rawbar(); if (st == 4) rawbar(); b1.issue(st + 3); }
            wait_vm<16>(); rawbar(); oa.issueW(0);
            wait_vm<12>(); rawbar(); oa.issueW(1);
            wait_vm<8>(); rawbar(); oa.issueW(2);
        }
        {
            LdOA oa; oa.init(P, obr, ksl, mt, nt, rot, C.lane, lw, C.L);
            rawbar();
            rawbar();
            oa.issueW(3);
            { const u64 e = *(LAS u64*)(uintptr_t)(C.L + SCR + 16384 + 1024);
              C.tag = ((u64)__builtin_amdgcn_readfirstlane((u32)(e >> 32)) << 32) | (u32)__builtin_amdgcn_readfirstlane((u32)e); }
            gu64* fp = C.fl + (size_t)((obr ? F_OB + (4 * mt + lw) * 32 : F_O + (2 * mt + (lw >> 1)) * 32) + ksl * 8 + (C.lane & 7)) * FS;
            u32 ready = 0;
            auto need = [&](int st) {
                const int hd = ((st & 8) | ((st + rot) & 7)) >> 1;
                for (u32 spins = 0; !((ready >> hd) & 1) && spins < SPIN_CAP; ++spins) {
                    if (spins) __builtin_amdgcn_s_sleep(POLL_SLEEP);
                    const u64 v = __hip_atomic_load(fp, RLX_AGENT);
                    ready = (u32)__ballot(v == C.tag) & 255u;
                }
                __builtin_amdgcn_fence(__ATOMIC_ACQUIRE, "wavefront");
            };
            need(0); oa.issueX(0); need(1); oa.issueX(1); need(2); oa.issueX(2); need(3); oa.issueX(3);

            wait_vm<12>(); rawbar();
            wait_vm<8>(); rawbar(); need(4); oa.issue(4);
            wait_vm<12>(); rawbar(); need(5); oa.issue(5);
#pragma unroll 1
            for (int st = 3; st < 13; ++st) { wait_vm<16>(); rawbar(); need(st + 3); oa.issue(st + 3); }
            wait_vm<16>(); rawbar(); wait_vm<8>(); rawbar(); wait_vm<0>(); rawbar();
        }
    }
    {
        const int br = obr;
        publish(C, F_P + ((br * 4 + mt) * 8 + nt) * 4 + ksl, 1);

        if (c == 128 && C.w == 4) {
            gu64* pa = C.fl + (size_t)(F_O + C.lane) * FS; gu64* pb = C.fl + (size_t)(F_Q + C.lane) * FS;
            for (u32 spins = 0; spins < SPIN_CAP; ++spins) {
                const u64 v0 = __hip_atomic_load(pa, RLX_AGENT), v1 = __hip_atomic_load(pa + 64 * FS, RLX_AGENT), v2 = __hip_atomic_load(pa + 128 * FS, RLX_AGENT), v3 = __hip_atomic_load(pa + 192 * FS, RLX_AGENT);
                const u64 v4 = __hip_atomic_load(pb, RLX_AGENT), v5 = __hip_atomic_load(pb + 64 * FS, RLX_AGENT);
                if (__all(v0 == C.tag && v1 == C.tag && v2 == C.tag && v3 == C.tag && v4 == C.tag && v5 == C.tag)) break;
                __builtin_amdgcn_s_sleep(POLL_SLEEP);
            }
            if (C.lane == 0) __hip_atomic_store(C.fl + F_EPOCH, C.tag, RLX_AGENT);
        }
        final_oa(C, P, br, ksl, mt, nt, ownA);
    }
}

extern "C" void kernel_launch(void* const* d_in, const int* in_sizes, int n_in,
                              void* d_out, int out_size, void* d_ws, size_t ws_size,
                              hipStream_t stream) {
    Params P;
    for (int br = 0; br < 2; ++br) {
        P.x[br] = (const u16*)d_in[br * 5 + 0]; P.k[br] = (const u16*)d_in[br * 5 + 1]; P.v[br] = (const u16*)d_in[br * 5 + 2];
        P.wq[br] = (const u16*)d_in[br * 5 + 3]; P.wo[br] = (const u16*)d_in[br * 5 + 4];
    }
    P.out = (u16*)d_out; P.ws = (char*)d_ws;
    (void)hipFuncSetAttribute((const void*)k_att, hipFuncAttributeMaxDynamicSharedMemorySize, LDS1_BYTES);
    k_att<<<256, 512, LDS1_BYTES, stream>>>(P);
}
```
